```python
import torch

def _softmax(x, axis=-1):
    return torch.softmax(x, dim=axis)
def _gelu(x):
    return torch.nn.functional.gelu(x, approximate="tanh")
def _silu(x):
    return torch.nn.functional.silu(x)
def _sigmoid(x):
    return torch.sigmoid(x)
def _layernorm(x, axis=-1, eps=1e-5):
    m = x.mean(dim=axis, keepdim=True)
    v = x.var(dim=axis, keepdim=True, unbiased=False)
    return (x - m) / torch.sqrt(v + eps)
def _scatter_add(tgt, idx, src, axis=0):
    return tgt.clone().index_add_(axis, idx, src)
def _segment_sum(src, seg, n):
    out = torch.zeros((n,) + src.shape[1:], dtype=src.dtype)
    return out.index_add_(0, seg, src)
def _pad(x, pw):
    flat = tuple(v for pair in reversed(pw) for v in pair)
    return torch.nn.functional.pad(x, flat)

kernel_name = 'standalone__self_sup_conv_stem_hubert__speech'

def setup_inputs():

    _w = (lambda a: (lambda t: t.to(torch.bfloat16) if t.is_floating_point() else t)(torch.as_tensor(a)))
    B, T, D, K = 2, 4000, 512, 5
    scale = (1.0 / (K * D)) ** 0.5
    return {
        "x": _w((lambda *s: torch.randn(*s, dtype=torch.bfloat16) * 0.05)(B, T, D)),
        "w": _w((lambda *s: torch.randn(*s, dtype=torch.bfloat16) * 0.05)(K, D, D) * scale),
        "b": _w((lambda *s: torch.randn(*s, dtype=torch.bfloat16) * 0.05)(D) * 0.1),
        "gamma": _w(torch.ones((D,), dtype=float)),
        "beta": _w(torch.zeros((D,), dtype=float)),
    }

def reference(x, w, b, gamma, beta):
    B, T, D = x.shape
    K = w.shape[0]
    s = 2
    L = (T - K) // s + 1
    y = torch.einsum('blc,cd->bld', x[:, 0 : s * L : s, :], w[0])
    for i in range(1, K):
        y = y + torch.einsum('blc,cd->bld', x[:, i : i + s * L : s, :], w[i])
    y = y + b[None, None, :]
    mu = y.mean(1, keepdims=True)
    var = ((y - mu) ** 2).mean(1, keepdims=True)
    yn = (y - mu) / torch.sqrt(var + 1e-5)
    yn = yn * gamma[None, None, :] + beta[None, None, :]
    return _gelu(yn)
```

```cpp
#include <hip/hip_runtime.h>
#include <hip/hip_bf16.h>
#include <stdint.h>

typedef __attribute__((ext_vector_type(8))) short bf16x8;
typedef __attribute__((ext_vector_type(16))) float f32x16;

constexpr int B_ = 2, T_ = 4000, D_ = 512, K_ = 5, L_ = 1998;

__device__ __forceinline__ float bf2f(unsigned short v) { return __uint_as_float(((unsigned)v) << 16); }

__global__ void __launch_bounds__(64) conv_naive(const unsigned short* __restrict__ x, const unsigned short* __restrict__ w,
                                                 const unsigned short* __restrict__ bias, float* __restrict__ y) {
  const int lane = threadIdx.x, r = lane & 31, h = lane >> 5;
  const int nt = blockIdx.x, mt = blockIdx.y, b = blockIdx.z;
  int l = mt * 32 + r; if (l > L_ - 1) l = L_ - 1;
  const unsigned short* xa = x + ((size_t)b * T_ + 2 * l) * D_;
  f32x16 acc = {};
  for (int k0 = 0; k0 < K_ * D_; k0 += 16) {
    bf16x8 a = *(const bf16x8*)(xa + k0 + 8 * h);
    bf16x8 bb;
    for (int j = 0; j < 8; ++j) bb[j] = (short)w[(size_t)(k0 + 8 * h + j) * D_ + nt * 32 + r];
    acc = __builtin_amdgcn_mfma_f32_32x32x16_bf16(a, bb, acc, 0, 0, 0);
  }
  const float bv = bf2f(bias[nt * 32 + r]);
  for (int g = 0; g < 16; ++g) {
    int row = mt * 32 + (g & 3) + 8 * (g >> 2) + 4 * h;
    if (row < L_) y[((size_t)b * L_ + row) * D_ + nt * 32 + r] = acc[g] + bv;
  }
}

__global__ void __launch_bounds__(256) stats_naive(const float* __restrict__ y, float* __restrict__ st) {
  __shared__ float red[256];
  const int d = blockIdx.x, b = blockIdx.y, t = threadIdx.x;
  float s = 0;
  for (int l = t; l < L_; l += 256) s += y[((size_t)b * L_ + l) * D_ + d];
  red[t] = s; __syncthreads();
  for (int o = 128; o > 0; o >>= 1) { if (t < o) red[t] += red[t + o]; __syncthreads(); }
  const float mu = red[0] / L_; __syncthreads();
  float q = 0;
  for (int l = t; l < L_; l += 256) { float v = y[((size_t)b * L_ + l) * D_ + d] - mu; q += v * v; }
  red[t] = q; __syncthreads();
  for (int o = 128; o > 0; o >>= 1) { if (t < o) red[t] += red[t + o]; __syncthreads(); }
  if (t == 0) { st[(b * D_ + d) * 2] = mu; st[(b * D_ + d) * 2 + 1] = 1.0f / sqrtf(red[0] / L_ + 1e-5f); }
}

__global__ void __launch_bounds__(256) norm_naive(const float* __restrict__ y, const float* __restrict__ st,
                                                  const unsigned short* __restrict__ gamma, const unsigned short* __restrict__ beta,
                                                  __hip_bfloat16* __restrict__ out) {
  const size_t i = (size_t)blockIdx.x * 256 + threadIdx.x;
  if (i >= (size_t)B_ * L_ * D_) return;
  const int d = i % D_, b = i / ((size_t)L_ * D_);
  float v = (y[i] - st[(b * D_ + d) * 2]) * st[(b * D_ + d) * 2 + 1] * bf2f(gamma[d]) + bf2f(beta[d]);
  const float u = 0.7978845608028654f * (v + 0.044715f * v * v * v);
  out[i] = __float2bfloat16(0.5f * v * (1.0f + tanhf(u)));
}

extern "C" void kernel_launch(void* const* d_in, const int* in_sizes, int n_in, void* d_out, int out_size, void* d_ws,
                              size_t ws_size, hipStream_t stream) {
  const unsigned short* x = (const unsigned short*)d_in[0];
  const unsigned short* w = (const unsigned short*)d_in[1];
  const unsigned short* bias = (const unsigned short*)d_in[2];
  const unsigned short* gamma = (const unsigned short*)d_in[3];
  const unsigned short* beta = (const unsigned short*)d_in[4];
  float* y = (float*)d_ws;
  float* st = y + (size_t)B_ * L_ * D_;
  conv_naive<<<dim3(D_ / 32, (L_ + 31) / 32, B_), 64, 0, stream>>>(x, w, bias, y);
  stats_naive<<<dim3(D_, B_), 256, 0, stream>>>(y, st);
  norm_naive<<<(B_ * L_ * D_ + 255) / 256, 256, 0, stream>>>(y, st, gamma, beta, (__hip_bfloat16*)d_out);
}
```

```cpp
#include <hip/hip_runtime.h>
#include <hip/hip_bf16.h>
#include <stdint.h>

typedef __attribute__((ext_vector_type(8))) short bf16x8;
typedef __attribute__((ext_vector_type(4))) short s16x4;
typedef __attribute__((ext_vector_type(4))) float f32x4;
typedef __attribute__((ext_vector_type(2))) float f32x2;
typedef __attribute__((ext_vector_type(4))) int i32x4;
#define LAS __attribute__((address_space(3)))
#define SBAR() __builtin_amdgcn_sched_barrier(0)

constexpr int T_IN = 4000, DM = 512, L_OUT = 1998, TILE_ROWS = 125;
constexpr int XSLOT = 32768, WSLOT = 20480;
constexpr int LDS_X = 0, LDS_W = 2 * XSLOT, LDS_ST = LDS_W + 4 * WSLOT, LDS_SC = LDS_ST + 1024, LDS_BYTES = LDS_SC + 512;
constexpr unsigned GRAN_BYTES = 256u * 512u;
constexpr unsigned long long GRAN_EMPTY = ~0ull;
constexpr unsigned SPIN_CAP = 1u << 15;
constexpr int POLL_DELAY = 18;
constexpr int GB = 2;
constexpr unsigned REANNOUNCE = 3;

__device__ __forceinline__ float bf2f(unsigned short v) { return __uint_as_float(((unsigned)v) << 16); }
__device__ __forceinline__ i32x4 make_srd(const void* p, unsigned bytes) {
  const uint64_t a = (uint64_t)p;
  i32x4 r;
  r[0] = __builtin_amdgcn_readfirstlane((int)(uint32_t)a);
  r[1] = __builtin_amdgcn_readfirstlane((int)(uint32_t)(a >> 32) & 0xffff);
  r[2] = (int)bytes;
  r[3] = 0x00020000;
  return r;
}
__device__ __forceinline__ void dma16(unsigned ldsaddr, i32x4 srd, unsigned voff, unsigned soff) {
  asm volatile("s_mov_b32 m0, %0\n\ts_nop 0\n\tbuffer_load_dwordx4 %1, %2, %3 offen lds" :: "s"(ldsaddr), "v"(voff), "s"(srd), "s"(soff) : "memory");
}
template <int N> __device__ __forceinline__ void wait_vm_bar() { asm volatile("s_waitcnt vmcnt(%0)\n\ts_barrier" :: "n"(N) : "memory"); }
__device__ __forceinline__ void wait_lgkm_bar() {
  __builtin_amdgcn_s_waitcnt(0xc07f);
  asm volatile("s_barrier" ::: "memory");
}
__device__ __forceinline__ float xsum16(float v) {
  auto s = __builtin_amdgcn_permlane16_swap(__float_as_uint(v), __float_as_uint(v), false, false);
  return __uint_as_float(s[0]) + __uint_as_float(s[1]);
}
__device__ __forceinline__ float xsum32(float v) {
  auto s = __builtin_amdgcn_permlane32_swap(__float_as_uint(v), __float_as_uint(v), false, false);
  return __uint_as_float(s[0]) + __uint_as_float(s[1]);
}
__device__ __forceinline__ float xsum(float v) { return xsum32(xsum16(v)); }
__device__ __forceinline__ float fold_rows(float a, float b) {
  auto s = __builtin_amdgcn_permlane16_swap(__float_as_uint(a), __float_as_uint(b), false, false);
  return xsum32(__uint_as_float(s[0]) + __uint_as_float(s[1]));
}
__device__ __forceinline__ unsigned cvtpk(float lo, float hi) {
  unsigned r; asm("v_cvt_pk_bf16_f32 %0, %1, %2" : "=v"(r) : "v"(lo), "v"(hi)); return r;
}

constexpr int AUX_AGENT = 16;
constexpr int AUX_OUT = 17;
__device__ __forceinline__ void st_agent(unsigned long long* p, unsigned long long v) { __hip_atomic_store(p, v, __ATOMIC_RELAXED, __HIP_MEMORY_SCOPE_AGENT); }
__device__ __forceinline__ __amdgpu_buffer_rsrc_t inbox_rsrc(unsigned long long* gran, int group) {
  return __builtin_amdgcn_make_buffer_rsrc((void*)((unsigned*)(gran + GRAN_BYTES / 8) + group * 512), 0, 2048, 0x00020000);
}
__device__ __forceinline__ void announce(unsigned long long mask, __amdgpu_buffer_rsrc_t rs, unsigned off, unsigned one) {
  asm volatile("s_waitcnt vmcnt(0)\n\ts_mov_b64 exec, %0\n\tbuffer_store_dword %1, %2, %3, 0 offen sc1\n\ts_mov_b64 exec, -1"
               :: "s"(mask), "v"(one), "v"(off), "s"(rs) : "memory");
}
struct Frag { bf16x8 A[11]; s16x4 B[5][2][2]; };
struct Addr { unsigned a[2][2]; unsigned b[2][2]; };

template <int XS, int S, int WS, int Q> __device__ __forceinline__ void frag_read(Frag& f, const Addr& ad) {
  constexpr int tap = Q < 8 ? 0 : Q < 16 ? 1 : Q < 21 ? 2 : Q < 26 ? 3 : 4;
  constexpr int j = Q < 8 ? Q : Q < 16 ? Q - 8 : Q < 21 ? Q - 16 : Q < 26 ? Q - 21 : Q - 26;
  if constexpr (j < 4) {
    constexpr int sn = j >> 1, h = j & 1;
    constexpr int off = (WS & 1) * WSLOT + tap * 4096 + h * 512;
    f.B[tap][sn][h] = __builtin_bit_cast(s16x4, __builtin_amdgcn_ds_read_tr16_b64_v4i16((LAS s16x4*)(ad.b[sn][WS >> 1] + off)));
  } else {
    constexpr int u = tap < 2 ? 2 * (j - 4) + tap : 6 + tap;
    constexpr int off = XS * XSLOT + (u & 7) * 4096;
    f.A[u] = *(const LAS bf16x8*)(ad.a[u >> 3][S] + off);
  }
}
template <int XS, int S, int WS, int Q> __device__ __forceinline__ void frag_read_all(Frag& f, const Addr& ad) {
  if constexpr (Q < 31) { frag_read<XS, S, WS, Q>(f, ad); frag_read_all<XS, S, WS, Q + 1>(f, ad); }
}
template <int XS, int S, int WS, bool PRE, int Q>
__device__ __forceinline__ void stage_steps(f32x4 (&acc)[4][2], const Frag& c, Frag& n, const Addr& ad) {
  if constexpr (Q < 40) {
    constexpr int i = Q >> 3, sn = Q & 1, t = (Q >> 1) & 3;
    if constexpr (PRE && Q < 31) frag_read<XS, S, WS, Q>(n, ad);
    const bf16x8 bb = __builtin_shufflevector(c.B[i][sn][0], c.B[i][sn][1], 0, 1, 2, 3, 4, 5, 6, 7);
    acc[t][sn] = __builtin_amdgcn_mfma_f32_16x16x32_bf16(c.A[2 * t + i], bb, acc[t][sn], 0, 0, 0);
    SBAR();
    stage_steps<XS, S, WS, PRE, Q + 1>(acc, c, n, ad);
  }
}

__global__ void __launch_bounds__(512, 2)
conv_stem_kernel(const unsigned short* __restrict__ x, const unsigned short* __restrict__ w, const unsigned short* __restrict__ bias,
                 const unsigned short* __restrict__ gamma, const unsigned short* __restrict__ beta, unsigned short* __restrict__ out,
                 unsigned long long* __restrict__ gran) {
  extern __shared__ __attribute__((aligned(16))) char lds[];
  asm volatile("" :: "s"(x), "s"(w), "s"(bias), "s"(gamma), "s"(beta), "s"(out), "s"(gran));
  const int lane = threadIdx.x & 63;
  const int wv = __builtin_amdgcn_readfirstlane(threadIdx.x >> 6);
  const int bid = blockIdx.x, xcd = bid & 7, idx8 = bid >> 3;
  const int mt = (xcd >> 1) * 8 + (idx8 & 7), nt = (xcd & 1) * 4 + (idx8 >> 3);
  const int b = mt >> 4, m = mt & 15;
  const int nvalid = m == 15 ? L_OUT - 15 * TILE_ROWS : TILE_ROWS;
  const unsigned lds0 = (unsigned)(uintptr_t)(LAS char*)lds;

  if (wv < 4) {
    unsigned long long* gran_m = gran;
    asm volatile("" : "+s"(gran_m));
    unsigned long long* const ggrp = gran_m + (size_t)(b * 8 + nt) * 16 * 64;
    const int wm = wv >> 1, wn = wv & 1;
    const int g4 = lane >> 4, n16 = lane & 15;
    Addr ad;
#pragma unroll
    for (int sh = 0; sh < 2; ++sh) {
      const unsigned ix = 16 * wm + n16 + sh, sw = ((ix >> 1) & 3) << 1;
#pragma unroll
      for (int s = 0; s < 2; ++s) ad.a[sh][s] = lds0 + LDS_X + ix * 128 + (((4 * s + g4) ^ sw) << 4);
    }
    {
      const unsigned row = 8 * g4 + ((lane >> 2) & 3), p = lane & 3, f2 = ((row >> 1) & 1) | (((row >> 3) & 1) << 1);
#pragma unroll
      for (int sn = 0; sn < 2; ++sn)
#pragma unroll
        for (int h = 0; h < 2; ++h)
          ad.b[sn][h] = lds0 + LDS_W + h * 2 * WSLOT + row * 128 + ((((wn * 2 + sn) ^ f2) * 2 + (p >> 1)) << 4) + (p & 1) * 8;
    }
    const int chw = nt * 64 + wn * 32 + n16;
    f32x4 acc[4][2];
#pragma unroll
    for (int sn = 0; sn < 2; ++sn)
#pragma unroll
      for (int t = 0; t < 4; ++t) acc[t][sn] = f32x4{0.f, 0.f, 0.f, 0.f};
    Frag f0, f1;
    const __amdgpu_buffer_rsrc_t hrs = inbox_rsrc(gran, b * 8 + nt);
    const unsigned long long hmask = wv == 3 ? 0xffffull : 0ull;
    unsigned hout = ((lane & 15) * 32 + m) * 4, hone = 1u;
    asm volatile("" : "+v"(hout), "+v"(hone));
    int chl = chw;
    asm volatile("" : "+v"(chl));
    const unsigned *bw = (const unsigned*)bias + (chl >> 1), *ew = (const unsigned*)beta + (chl >> 1), *gw = (const unsigned*)gamma + (chl >> 1);
    unsigned rp0 = bw[0], rp1 = bw[8], rp2 = ew[0], rp3 = ew[8], rp4 = gw[0], rp5 = gw[8];
    if (wv == 3) {
      st_agent(ggrp + m * 64 + lane, GRAN_EMPTY);
      __builtin_amdgcn_raw_buffer_store_b32(0u, hrs, (m * 32 + (lane & 15)) * 4, 0, AUX_AGENT);
    }
    const int lim = (wm == 1 && g4 == 3) ? nvalid - 112 : 16;
    unsigned keepA = lim > 12 ? ~0u : 0u, keepB = lim > 13 ? ~0u : 0u;
    float rcnt = wm == 0 ? 1.0f / 64 : m == 15 ? 1.0f / (L_OUT - 15 * TILE_ROWS - 64) : 1.0f / (TILE_ROWS - 64);
    unsigned vo = (unsigned)(b * L_OUT + TILE_ROWS * m + 64 * wm + 16 * g4) * 1024 + nt * 128 + wn * 64 + ((n16 & 1) * 16 + (n16 & ~1)) * 2;
    unsigned voA = lim > 12 ? vo : 0x80000000u, voB = lim > 13 ? vo : 0x80000000u;
    unsigned psel = (n16 & 1) ? 0x03020706u : 0x05040100u;
    unsigned hsel = (n16 & 1) ? 0x07060c0cu : 0x05040c0cu;
    asm volatile("" : "+v"(keepA), "+v"(keepB), "+v"(rcnt), "+v"(vo), "+v"(voA), "+v"(voB), "+v"(psel), "+v"(hsel));
    asm volatile("" : "+v"(ad.a[0][0]), "+v"(ad.a[0][1]), "+v"(ad.a[1][0]), "+v"(ad.a[1][1]), "+v"(ad.b[0][0]), "+v"(ad.b[0][1]), "+v"(ad.b[1][0]), "+v"(ad.b[1][1]));
    asm volatile("s_barrier" ::: "memory");
    frag_read_all<0, 0, 0, 0>(f0, ad);
#pragma nounroll
    for (int it = 0; it < 3; ++it) {
      wait_lgkm_bar(); stage_steps<0, 1, 1, true, 0>(acc, f0, f1, ad);
      wait_lgkm_bar(); stage_steps<1, 0, 2, true, 0>(acc, f1, f0, ad);
      wait_lgkm_bar(); stage_steps<1, 1, 3, true, 0>(acc, f0, f1, ad);
      wait_lgkm_bar(); stage_steps<0, 0, 0, true, 0>(acc, f1, f0, ad);
    }
    asm volatile("" : "+v"(rp0), "+v"(rp1), "+v"(rp2), "+v"(rp3), "+v"(rp4), "+v"(rp5));
    auto half = [&](unsigned v) { return __uint_as_float(__builtin_amdgcn_perm(v, 0u, hsel)); };
    float pb0 = half(rp0), pb1 = half(rp1), pe0 = half(rp2), pe1 = half(rp3), pg0 = half(rp4), pg1 = half(rp5);
    asm volatile("" : "+v"(pb0), "+v"(pb1), "+v"(pe0), "+v"(pe1), "+v"(pg0), "+v"(pg1));
    float pbl = (g4 & 1) ? pb1 : pb0;
    asm volatile("" : "+v"(pbl));
    announce(hmask, hrs, hout, hone);
    SBAR();
    wait_lgkm_bar(); stage_steps<0, 1, 1, true, 0>(acc, f0, f1, ad);
    wait_lgkm_bar(); stage_steps<1, 0, 2, true, 0>(acc, f1, f0, ad);
    wait_lgkm_bar(); stage_steps<1, 1, 3, true, 0>(acc, f0, f1, ad);
    SBAR();
    wait_lgkm_bar(); stage_steps<0, 0, 0, false, 0>(acc, f1, f0, ad);

    auto keep = [&](float v, int q) { return q < 11 ? v : __uint_as_float(__float_as_uint(v) & (q < 13 ? keepA : keepB)); };
    float mean_w[2], part[2];
#pragma unroll
    for (int sn = 0; sn < 2; ++sn) {
#pragma unroll
      for (int t = 0; t < 4; ++t) { acc[t][sn][2] = keep(acc[t][sn][2], 8 + t); acc[t][sn][3] = keep(acc[t][sn][3], 12 + t); }
      f32x2 u[4];
#pragma unroll
      for (int t = 0; t < 4; ++t) u[t] = f32x2{acc[t][sn][0], acc[t][sn][1]} + f32x2{acc[t][sn][2], acc[t][sn][3]};
      const f32x2 v = (u[0] + u[1]) + (u[2] + u[3]);
      part[sn] = v[0] + v[1];
    }
    const float mean_l = fold_rows(part[0], part[1]) * rcnt;
    {
      auto sw = __builtin_amdgcn_permlane16_swap(__float_as_uint(mean_l), __float_as_uint(mean_l), false, false);
      mean_w[0] = __uint_as_float(sw[0]); mean_w[1] = __uint_as_float(sw[1]);
    }
#pragma unroll
    for (int sn = 0; sn < 2; ++sn) {
      const f32x2 mm = {mean_w[sn], mean_w[sn]};
      f32x2 qa = {0.f, 0.f}, qb = {0.f, 0.f};
#pragma unroll
      for (int t = 0; t < 4; ++t) {
        const f32x2 lo = f32x2{acc[t][sn][0], acc[t][sn][1]} - mm;
        f32x2 hi = f32x2{acc[t][sn][2], acc[t][sn][3]} - mm;
        hi[0] = keep(hi[0], 8 + t); hi[1] = keep(hi[1], 12 + t);
        acc[t][sn] = f32x4{lo[0], lo[1], hi[0], hi[1]};
        qa = __builtin_elementwise_fma(lo, lo, qa);
        qb = __builtin_elementwise_fma(hi, hi, qb);
      }
      const f32x2 qq = qa + qb;
      part[sn] = qq[0] + qq[1];
    }
    const float m2_l = fold_rows(part[0], part[1]);
    const float meanb_l = mean_l + pbl;
    {
      LAS float* st = (LAS float*)(lds0 + LDS_ST);
      if (lane < 32) {
        const int c = wn * 32 + lane;
        st[wm * 128 + c] = meanb_l;
        st[wm * 128 + 64 + c] = m2_l;
      }
    }
    wait_lgkm_bar();
    if (wm == 0 && lane < 32) {
      LAS float* st = (LAS float*)(lds0 + LDS_ST);
      const int c = wn * 32 + lane;
      const float ma = st[c], qa = st[64 + c], mb = st[128 + c], qb = st[192 + c];
      const float nb = (float)(nvalid - 64), inv = m == 15 ? 1.0f / (L_OUT - 15 * TILE_ROWS) : 1.0f / TILE_ROWS, d = mb - ma;
      const float mean = ma + d * nb * inv;
      unsigned m2b = __float_as_uint(qa + qb + d * d * 64.f * nb * inv);
      m2b = m2b == 0xffffffffu ? 0x7fc00000u : m2b;
      st_agent(ggrp + m * 64 + c, ((unsigned long long)m2b << 32) | __float_as_uint(mean));
    }
    wait_lgkm_bar();
    float scale[2], shift[2];
    {
      LAS float* sc = (LAS float*)(lds0 + LDS_SC) + wn * 32 + n16;
      scale[0] = sc[64] * pg0; scale[1] = sc[80] * pg1;
      shift[0] = __builtin_fmaf(mean_w[0] + pb0 - sc[0], scale[0], pe0);
      shift[1] = __builtin_fmaf(mean_w[1] + pb1 - sc[16], scale[1], pe1);
    }
    const __amdgpu_buffer_rsrc_t ors = __builtin_amdgcn_make_buffer_rsrc((void*)out, 0, 2 * L_OUT * 1024, 0x00020000);
    constexpr float C1 = -2.0f * 1.4426950408889634f * 0.7978845608028654f, C3 = C1 * 0.044715f;
#pragma unroll
    for (int bt = 0; bt < 16 / GB; ++bt) {
      f32x2 v[GB], a[GB];
#pragma unroll
      for (int i = 0; i < GB; ++i) {
        const int p = bt * GB + i, sn = p & 1, t = (p >> 1) & 3, jp = p >> 3;
        v[i] = __builtin_elementwise_fma(f32x2{acc[t][sn][2 * jp], acc[t][sn][2 * jp + 1]}, f32x2{scale[sn], scale[sn]}, f32x2{shift[sn], shift[sn]});
      }
#pragma unroll
      for (int i = 0; i < GB; ++i) a[i] = v[i] * v[i];
#pragma unroll
      for (int i = 0; i < GB; ++i) a[i] = __builtin_elementwise_fma(a[i], f32x2{C3, C3}, f32x2{C1, C1});
#pragma unroll
      for (int i = 0; i < GB; ++i) a[i] = a[i] * v[i];
#pragma unroll
      for (int i = 0; i < GB; ++i) a[i] = f32x2{__builtin_amdgcn_exp2f(a[i][0]), __builtin_amdgcn_exp2f(a[i][1])};
#pragma unroll
      for (int i = 0; i < GB; ++i) a[i] = a[i] + f32x2{1.0f, 1.0f};
#pragma unroll
      for (int i = 0; i < GB; ++i) a[i] = f32x2{__builtin_amdgcn_rcpf(a[i][0]), __builtin_amdgcn_rcpf(a[i][1])};
#pragma unroll
      for (int i = 0; i < GB; ++i) a[i] = a[i] * v[i];
      unsigned pk[GB], ot[GB];
#pragma unroll
      for (int i = 0; i < GB; ++i) pk[i] = cvtpk(a[(i & ~1)][i & 1], a[(i & ~1) + 1][i & 1]);
#pragma unroll
      for (int i = 0; i < GB; ++i) ot[i] = (unsigned)__builtin_amdgcn_update_dpp(0, (int)pk[i], 0xB1, 0xf, 0xf, true);
#pragma unroll
      for (int i = 0; i < GB; ++i) {
        const int p = bt * GB + (i & ~1), t = (p >> 1) & 3, j = 2 * (p >> 3) + (i & 1), q = 4 * j + t;
        const unsigned r = __builtin_amdgcn_perm(ot[i], pk[i], psel);
        __builtin_amdgcn_raw_buffer_store_b32(r, ors, (q < 11 ? vo : q < 13 ? voA : voB) + t * 1024, j * 4096, AUX_OUT);
      }
    }
    return;
  }

  {
    const int lw = wv - 4;
    const unsigned r8 = lane >> 3, c8 = lane & 7;
    const i32x4 xs = make_srd(x, 2u * T_IN * 1024u), wsr = make_srd(w, 2560u * 1024u);
    const int rot = xcd;
    unsigned vx[8];
    {
      const unsigned ix = 8 * lw + r8, sw = ((ix >> 1) & 3) << 1;
      const unsigned r0 = b * T_IN + 2 * TILE_ROWS * m + 8 * ix;
#pragma unroll
      for (int p = 0; p < 8; ++p) {
        unsigned r = r0 + p; r = r > 2 * T_IN - 1 ? 2 * T_IN - 1 : r;
        vx[p] = r * 1024 + ((c8 ^ sw) << 4);
      }
    }
    const unsigned kk = 8 * lw + r8;
    const unsigned vw = kk * 1024 + nt * 128 + ((c8 ^ ((((kk >> 1) & 1) | (((kk >> 3) & 1) << 1)) << 1)) << 4);
    const unsigned xdst = lds0 + LDS_X + lw * 1024, wdst = lds0 + LDS_W + lw * 1024;
    auto issue_x = [&](int c) {
      const unsigned so = ((c + rot) & 7) * 128, d = xdst + (c & 1) * XSLOT;
#pragma unroll
      for (int p = 0; p < 8; ++p) dma16(d + p * 4096, xs, vx[p], so);
    };
    auto issue_w = [&](int n) {
      const unsigned so = ((((n >> 1) + rot) & 7) * 64 + (n & 1) * 32) * 1024, d = wdst + (n & 3) * WSLOT;
#pragma unroll
      for (int i = 0; i < 5; ++i) dma16(d + i * 4096, wsr, vw, so + i * 512 * 1024);
    };
    issue_x(0); issue_w(0);
    issue_w(1); issue_x(1);
    wait_vm_bar<13>();
    issue_w(2); issue_w(3);
    for (int c = 0; c < 6; ++c) {
      wait_vm_bar<18>();
      issue_w(2 * c + 4);
      wait_vm_bar<10>();
      issue_x(c + 2); issue_w(2 * c + 5);
    }
    wait_vm_bar<18>();
    wait_vm_bar<5>();
    wait_vm_bar<0>();
    unsigned long long* gran_h = gran;
    asm volatile("" : "+s"(gran_h));
    const __amdgpu_buffer_rsrc_t hrs = inbox_rsrc(gran_h, b * 8 + nt);
    const unsigned hin = (m * 32 + (lane & 15)) * 4, hout = ((lane & 15) * 32 + m) * 4;
    unsigned hmsg = 1u;
    if (lw == 0) hmsg = __builtin_amdgcn_raw_buffer_load_b32(hrs, hin, 0, AUX_AGENT);
    asm volatile("s_barrier" ::: "memory");
    for (unsigned spins = 0; !__all(hmsg != 0u) && spins < SPIN_CAP; ++spins) {
      __builtin_amdgcn_raw_buffer_store_b32(1u, hrs, hout, 0, AUX_AGENT);
      hmsg = __builtin_amdgcn_raw_buffer_load_b32(hrs, hin, 0, AUX_AGENT);
    }
    asm volatile("s_barrier" ::: "memory");
    unsigned long long* gran_l = gran;
    asm volatile("" : "+s"(gran_l));
    unsigned long long* const ggrp = gran_l + (size_t)(b * 8 + nt) * 16 * 64;
    constexpr int NG = 4;
    const int pg = lane >> 4, n = lane & 15, ch = 16 * lw + n;
    unsigned long long g[NG];
    unsigned long long* const gp = ggrp + NG * pg * 64 + ch;
    unsigned spins = 0;
    bool ok;
    __builtin_amdgcn_s_sleep(POLL_DELAY);
    do {
      ok = true;
#pragma unroll
      for (int k = 0; k < NG; ++k) g[k] = __hip_atomic_load(gp + k * 64, __ATOMIC_RELAXED, __HIP_MEMORY_SCOPE_AGENT);
#pragma unroll
      for (int k = 0; k < NG; ++k) ok &= (unsigned)(g[k] >> 32) != 0xffffffffu;
      if (spins >= REANNOUNCE && lw == 0) __builtin_amdgcn_raw_buffer_store_b32(1u, hrs, hout, 0, AUX_AGENT);
    } while (!__all(ok) && ++spins < SPIN_CAP);
    float s = 0.f, mean_p[NG], cnt[NG];
#pragma unroll
    for (int k = 0; k < NG; ++k) {
      const float last = (float)(L_OUT - 15 * TILE_ROWS), full = (float)TILE_ROWS;
      cnt[k] = (k == NG - 1 && pg == 3) ? last : full;
      mean_p[k] = __uint_as_float((unsigned)g[k]);
      s = __builtin_fmaf(cnt[k], mean_p[k], s);
    }
    const float mu = xsum(s) * (1.0f / L_OUT);
    float q = 0.f, q1 = 0.f;
#pragma unroll
    for (int k = 0; k < NG; ++k) { const float d = mean_p[k] - mu; q += __uint_as_float((unsigned)(g[k] >> 32)); q1 = __builtin_fmaf(cnt[k] * d, d, q1); }
    const float var = xsum(q + q1) * (1.0f / L_OUT);
    const float rstd = __builtin_amdgcn_rsqf(var + 1e-5f);
    if (pg == 0) {
      LAS float* sc = (LAS float*)(lds0 + LDS_SC) + ch;
      sc[0] = mu; sc[64] = rstd;
    }
    wait_lgkm_bar();
  }
}


extern "C" void kernel_launch(void* const* d_in, const int* in_sizes, int n_in, void* d_out, int out_size, void* d_ws,
                              size_t ws_size, hipStream_t stream) {
  (void)in_sizes; (void)n_in; (void)out_size; (void)ws_size;
  (void)hipFuncSetAttribute((const void*)conv_stem_kernel, hipFuncAttributeMaxDynamicSharedMemorySize, LDS_BYTES);
  conv_stem_kernel<<<256, 512, LDS_BYTES, stream>>>(
      (const unsigned short*)d_in[0], (const unsigned short*)d_in[1], (const unsigned short*)d_in[2],
      (const unsigned short*)d_in[3], (const unsigned short*)d_in[4], (unsigned short*)d_out, (unsigned long long*)d_ws);
}
```
